```python
import torch

def _softmax(x, axis=-1):
    return torch.softmax(x, dim=axis)
def _gelu(x):
    return torch.nn.functional.gelu(x, approximate="tanh")
def _silu(x):
    return torch.nn.functional.silu(x)
def _sigmoid(x):
    return torch.sigmoid(x)
def _layernorm(x, axis=-1, eps=1e-5):
    m = x.mean(dim=axis, keepdim=True)
    v = x.var(dim=axis, keepdim=True, unbiased=False)
    return (x - m) / torch.sqrt(v + eps)
def _scatter_add(tgt, idx, src, axis=0):
    return tgt.clone().index_add_(axis, idx, src)
def _segment_sum(src, seg, n):
    out = torch.zeros((n,) + src.shape[1:], dtype=src.dtype)
    return out.index_add_(0, seg, src)
def _pad(x, pw):
    flat = tuple(v for pair in reversed(pw) for v in pair)
    return torch.nn.functional.pad(x, flat)

kernel_name = 'standalone__instancenorm__prompt'

def setup_inputs():

    _w = (lambda a: (lambda t: t.to(torch.bfloat16) if t.is_floating_point() else t)(torch.as_tensor(a)))
    B, C, H, W = 2, 448, 2048, 16
    return {
        "x": _w(abs((lambda *s: torch.randn(*s, dtype=torch.bfloat16) * 0.05)(B, C, H, W))),
        "g": _w((lambda *s: torch.randn(*s, dtype=torch.bfloat16) * 0.05)(C) + 1.0),
        "b": _w((lambda *s: torch.randn(*s, dtype=torch.bfloat16) * 0.4)(C)),
    }

def reference(x, g, b):
    mu = x.mean(axis=(-2, -1), keepdims=True)
    var = ((x - mu) ** 2).mean(axis=(-2, -1), keepdims=True)
    xhat = (x - mu) / torch.sqrt(var + 1e-5)
    return xhat * g.reshape(1, -1, 1, 1) + b.reshape(1, -1, 1, 1)
```

```cpp
#include <hip/hip_runtime.h>
#include <hip/hip_bf16.h>
#include <stdint.h>

typedef unsigned int u32x4 __attribute__((ext_vector_type(4)));
typedef float f32x2 __attribute__((ext_vector_type(2)));

#define IN_THREADS 512
#define IN_WPE (IN_THREADS / 64)
#define IN_NT_LOAD 1

static constexpr int kHW = 2048 * 16;

__device__ __forceinline__ float bf_lo(unsigned int u) { return __uint_as_float(u << 16); }
__device__ __forceinline__ float bf_hi(unsigned int u) { return __uint_as_float(u & 0xffff0000u); }

__device__ __forceinline__ float wave_sum(float v) {
  int i;
  i = __builtin_amdgcn_update_dpp(0, __float_as_int(v), 0x111, 0xf, 0xf, false); v += __int_as_float(i);
  i = __builtin_amdgcn_update_dpp(0, __float_as_int(v), 0x112, 0xf, 0xf, false); v += __int_as_float(i);
  i = __builtin_amdgcn_update_dpp(0, __float_as_int(v), 0x114, 0xf, 0xf, false); v += __int_as_float(i);
  i = __builtin_amdgcn_update_dpp(0, __float_as_int(v), 0x118, 0xf, 0xf, false); v += __int_as_float(i);
  i = __builtin_amdgcn_update_dpp(0, __float_as_int(v), 0x142, 0xa, 0xf, false); v += __int_as_float(i);
  i = __builtin_amdgcn_update_dpp(0, __float_as_int(v), 0x143, 0xc, 0xf, false); v += __int_as_float(i);
  return __int_as_float(__builtin_amdgcn_readlane(__float_as_int(v), 63));
}

__device__ __forceinline__ unsigned int pack_bf16x2(float lo, float hi) {
  typedef __bf16 bf16x2_t __attribute__((ext_vector_type(2)));
  bf16x2_t r;
  r.x = (__bf16)lo;
  r.y = (__bf16)hi;
  return __builtin_bit_cast(unsigned int, r);
}

template <int T, int HW>
__global__ __launch_bounds__(T, IN_WPE) void instnorm_reg_kernel(const u32x4* __restrict__ x,
                                                         const unsigned short* __restrict__ gam,
                                                         const unsigned short* __restrict__ bet,
                                                         u32x4* __restrict__ y, int C) {
  constexpr int NW = T / 64;
  constexpr int NL = HW / 8 / T;
  static_assert(HW % (8 * T) == 0, "HW must be a multiple of 8*T");
  __shared__ float red[2][NW];

  const int inst = blockIdx.x;
  const int t = threadIdx.x;
  const int wv = t >> 6;
  const size_t base = (size_t)inst * (HW / 8) + t;
  const u32x4* xp = x + base;
  u32x4* yp = y + base;

  u32x4 v[NL];
#pragma unroll
  for (int j = 0; j < NL; ++j) {
    v[j] = __builtin_nontemporal_load(xp + j * T);
  }
  __builtin_amdgcn_sched_barrier(0);

  const int c = inst % C;
  unsigned int graw, braw;
  if ((c | 1) < C && ((((uintptr_t)gam | (uintptr_t)bet) & 3u) == 0u)) {
    const unsigned int gw = ((const unsigned int*)gam)[c >> 1];
    const unsigned int bw = ((const unsigned int*)bet)[c >> 1];
    graw = (c & 1) ? (gw & 0xffff0000u) : (gw << 16);
    braw = (c & 1) ? (bw & 0xffff0000u) : (bw << 16);
  } else {
    graw = (unsigned int)gam[c] << 16;
    braw = (unsigned int)bet[c] << 16;
  }

  f32x2 acc0 = {0.f, 0.f}, acc1 = {0.f, 0.f};
#pragma unroll
  for (int j = 0; j < NL; ++j) {
    f32x2 a = {bf_lo(v[j].x), bf_hi(v[j].x)};
    f32x2 b = {bf_lo(v[j].y), bf_hi(v[j].y)};
    f32x2 c = {bf_lo(v[j].z), bf_hi(v[j].z)};
    f32x2 d = {bf_lo(v[j].w), bf_hi(v[j].w)};
    acc0 += a; acc1 += b; acc0 += c; acc1 += d;
  }
  acc0 += acc1;
  float s = wave_sum(acc0.x + acc0.y);
  if ((t & 63) == 0) red[0][wv] = s;
  __syncthreads();
  float tot = 0.f;
#pragma unroll
  for (int w = 0; w < NW; ++w) tot += red[0][w];
  const float mean = tot * (1.0f / (float)HW);

#pragma unroll
  for (int j = 0; j < NL; ++j) asm volatile("" : "+v"(v[j]));

  const f32x2 m2 = {mean, mean};
  acc0 = (f32x2){0.f, 0.f}; acc1 = (f32x2){0.f, 0.f};
#pragma unroll
  for (int j = 0; j < NL; ++j) {
    f32x2 a = {bf_lo(v[j].x), bf_hi(v[j].x)};
    f32x2 b = {bf_lo(v[j].y), bf_hi(v[j].y)};
    f32x2 c = {bf_lo(v[j].z), bf_hi(v[j].z)};
    f32x2 d = {bf_lo(v[j].w), bf_hi(v[j].w)};
    a -= m2; b -= m2; c -= m2; d -= m2;
    acc0 += a * a; acc1 += b * b; acc0 += c * c; acc1 += d * d;
  }
  acc0 += acc1;
  float q = wave_sum(acc0.x + acc0.y);
  if ((t & 63) == 0) red[1][wv] = q;
  __syncthreads();
  float totq = 0.f;
#pragma unroll
  for (int w = 0; w < NW; ++w) totq += red[1][w];
  const float var = totq * (1.0f / (float)HW);
  const float rstd = 1.0f / sqrtf(var + 1e-5f);

  const float gv = __uint_as_float(graw);
  const float bv = __uint_as_float(braw);
  const float sc = rstd * gv;
  const f32x2 sc2 = {sc, sc};
  const f32x2 bv2 = {bv, bv};

#pragma unroll
  for (int j = 0; j < NL; ++j) asm volatile("" : "+v"(v[j]));

#pragma unroll
  for (int j = 0; j < NL; ++j) {
    f32x2 a = {bf_lo(v[j].x), bf_hi(v[j].x)};
    f32x2 b = {bf_lo(v[j].y), bf_hi(v[j].y)};
    f32x2 cc = {bf_lo(v[j].z), bf_hi(v[j].z)};
    f32x2 d = {bf_lo(v[j].w), bf_hi(v[j].w)};
    a = (a - m2) * sc2 + bv2;
    b = (b - m2) * sc2 + bv2;
    cc = (cc - m2) * sc2 + bv2;
    d = (d - m2) * sc2 + bv2;
    u32x4 o;
    o.x = pack_bf16x2(a.x, a.y);
    o.y = pack_bf16x2(b.x, b.y);
    o.z = pack_bf16x2(cc.x, cc.y);
    o.w = pack_bf16x2(d.x, d.y);
    yp[j * T] = o;
  }
}

extern "C" void kernel_launch(void* const* d_in, const int* in_sizes, int n_in,
                              void* d_out, int out_size, void* d_ws, size_t ws_size,
                              hipStream_t stream) {
  const u32x4* x = (const u32x4*)d_in[0];
  const unsigned short* g = (const unsigned short*)d_in[1];
  const unsigned short* b = (const unsigned short*)d_in[2];
  u32x4* y = (u32x4*)d_out;
  const int C = in_sizes[1];
  const int ninst = in_sizes[0] / kHW;
  instnorm_reg_kernel<IN_THREADS, kHW><<<ninst, IN_THREADS, 0, stream>>>(x, g, b, y, C);
}
```
